```python
import torch

def _softmax(x, axis=-1):
    return torch.softmax(x, dim=axis)
def _gelu(x):
    return torch.nn.functional.gelu(x, approximate="tanh")
def _silu(x):
    return torch.nn.functional.silu(x)
def _sigmoid(x):
    return torch.sigmoid(x)
def _layernorm(x, axis=-1, eps=1e-5):
    m = x.mean(dim=axis, keepdim=True)
    v = x.var(dim=axis, keepdim=True, unbiased=False)
    return (x - m) / torch.sqrt(v + eps)
def _scatter_add(tgt, idx, src, axis=0):
    return tgt.clone().index_add_(axis, idx, src)
def _segment_sum(src, seg, n):
    out = torch.zeros((n,) + src.shape[1:], dtype=src.dtype)
    return out.index_add_(0, seg, src)
def _pad(x, pw):
    flat = tuple(v for pair in reversed(pw) for v in pair)
    return torch.nn.functional.pad(x, flat)

kernel_name = 'interleave_layers__linear_kernel_hedgehog__mixer_s_mlp__prompt'

def setup_inputs():

    _w = (lambda a: (lambda t: t.to(torch.bfloat16) if t.is_floating_point() else t)(torch.as_tensor(a)))
    def _A_setup():
        B, S, D, H = 1, 2048, 448, 16
        Dh = D // H
        return {
            "x": _w((lambda *s: torch.randn(*s, dtype=torch.bfloat16) * 0.05)(B, S, D)),
            "wqkv": _w((lambda *s: torch.randn(*s, dtype=torch.bfloat16) * 0.4)(D, 3 * D)),
            "w_phi": _w((lambda *s: torch.randn(*s, dtype=torch.bfloat16) * 0.4)(H, Dh, Dh) * (Dh ** -0.5)),
            "wo": _w((lambda *s: torch.randn(*s, dtype=torch.bfloat16) * 0.05)(D, D)),
        }
    _A_in = _A_setup()

    def _B_setup():
        B, S, D, F = 1, 2048, 448, 1792
        return {
            "x":  _w((lambda *s: torch.randn(*s, dtype=torch.bfloat16) * 0.05)(B, S, D)),
            "w1": _w((lambda *s: torch.randn(*s, dtype=torch.bfloat16) * 3.2)(D, D) * (D ** -0.5)),
            "w2": _w((lambda *s: torch.randn(*s, dtype=torch.bfloat16) * 3.2)(D, D) * (D ** -0.5)),
            "w3": _w((lambda *s: torch.randn(*s, dtype=torch.bfloat16) * 3.2)(D, F) * (D ** -0.5)),
            "w4": _w((lambda *s: torch.randn(*s, dtype=torch.bfloat16) * 3.2)(F, D) * (F ** -0.5)),
        }
    _B_in = _B_setup()

    return {
        "A_x": _A_in["x"],
        "A_wqkv": _A_in["wqkv"],
        "A_w_phi": _A_in["w_phi"],
        "A_wo": _A_in["wo"],
        "B_x": _B_in["x"],
        "B_w1": _B_in["w1"],
        "B_w2": _B_in["w2"],
        "B_w3": _B_in["w3"],
        "B_w4": _B_in["w4"],
    }

def reference(A_x, A_wqkv, A_w_phi, A_wo, B_x, B_w1, B_w2, B_w3, B_w4):
    def _A_ref(x, wqkv, w_phi, wo):
        B, S, D = x.shape
        H = 16; Dh = D // H
        qkv = (lambda x, *d: x.permute(*d))((x @ wqkv).reshape(B, S, 3, H, Dh), 2, 0, 3, 1, 4)
        q, k, v = qkv[0], qkv[1], qkv[2]
        pq = torch.einsum("bhsd,hdf->bhsf", q, w_phi)
        pk = torch.einsum("bhsd,hdf->bhsf", k, w_phi)
        phi_q = torch.concatenate([_softmax(pq, axis=-1), _softmax(-pq, axis=-1)], axis=-1)
        phi_k = torch.concatenate([_softmax(pk, axis=-1), _softmax(-pk, axis=-1)], axis=-1)
        kv = torch.einsum("bhsf,bhsd->bhfd", phi_k, v)
        z  = torch.einsum("bhsf->bhf", phi_k)
        num = torch.einsum("bhqf,bhfd->bhqd", phi_q, kv)
        den = torch.einsum("bhqf,bhf->bhq", phi_q, z)[..., None] + 1e-6
        o = num / den
        return (lambda x, *d: x.permute(*d))(o, 0, 2, 1, 3).reshape(B, S, D) @ wo

    def _B_ref(x, w1, w2, w3, w4):
        B, S, D = x.shape
        h = _gelu(_layernorm(x, axis=-1) @ w1)
        c = D // 4
        g0 = _pad(h[:, :, 0*c:1*c], ((0, 0), (1, 0), (0, 0)))[:, :S, :]
        g1 = _pad(h[:, :, 1*c:2*c], ((0, 0), (0, 1), (0, 0)))[:, 1:, :]
        g2 = _pad(h[:, :, 2*c:3*c], ((0, 0), (2, 0), (0, 0)))[:, :S, :]
        g3 = _pad(h[:, :, 3*c:   ], ((0, 0), (0, 2), (0, 0)))[:, 2:, :]
        hs = torch.concatenate([g0, g1, g2, g3], axis=-1) @ w2
        x1 = x + hs
        h2 = _gelu(_layernorm(x1, axis=-1) @ w3) @ w4
        return x1 + h2

    x = A_x
    x = x + _A_ref(_layernorm(x, axis=-1), A_wqkv, A_w_phi, A_wo)
    x = x + _B_ref(_layernorm(x, axis=-1), B_w1, B_w2, B_w3, B_w4)
    x = x + _A_ref(_layernorm(x, axis=-1), A_wqkv, A_w_phi, A_wo)
    x = x + _B_ref(_layernorm(x, axis=-1), B_w1, B_w2, B_w3, B_w4)
    return x
```

```cpp
#include <hip/hip_runtime.h>
#include <hip/hip_bf16.h>
#include <stdint.h>

constexpr int S_ = 2048, D_ = 448, F_ = 1792;
constexpr int KVSZ = 29 * 56;

typedef unsigned short u16;
typedef unsigned long long u64;
typedef __attribute__((ext_vector_type(8))) short bf16x8;
typedef __attribute__((ext_vector_type(4))) short s16x4;
typedef __attribute__((ext_vector_type(4))) float f32x4;
typedef __attribute__((ext_vector_type(4))) unsigned u32x4;
typedef __attribute__((ext_vector_type(2))) unsigned u32x2;
typedef __attribute__((ext_vector_type(2))) __bf16 bf2_t;
typedef __attribute__((ext_vector_type(2))) float f2_t;
#define LAS __attribute__((address_space(3)))
#define GAS __attribute__((address_space(1)))
#define DI __device__ __forceinline__
#define RLX_AGENT __ATOMIC_RELAXED, __HIP_MEMORY_SCOPE_AGENT
typedef GAS u64 gu64;

constexpr unsigned OFF_FLAGS = 4096;
constexpr unsigned OFF_KVP = 65536;
constexpr unsigned KVP_PAIR = 256u * KVSZ * 2u;
constexpr unsigned ACT = (unsigned)S_ * D_ * 2u;
constexpr unsigned OFF_IMG = 2u << 20;
constexpr unsigned OFF_ACT = 4u << 20;
constexpr unsigned STB = (unsigned)S_ * 128u;
constexpr unsigned PAIR_BYTES = 5u * ACT + 4u * ACT + 3u * STB;
constexpr unsigned WS_USED = OFF_ACT + 2u * PAIR_BYTES;

DI unsigned pack2(float a, float b) {
  f2_t v = {a, b};
  bf2_t r = __builtin_convertvector(v, bf2_t);
  return __builtin_bit_cast(unsigned, r);
}
DI float bflo(unsigned u) { return __uint_as_float(u << 16); }
DI float bfhi(unsigned u) { return __uint_as_float(u & 0xffff0000u); }
DI float rbf(float a) { return bflo(pack2(a, 0.f) & 0xffffu); }

DI f32x4 mfma16(bf16x8 a, bf16x8 b, f32x4 c) {
  return __builtin_amdgcn_mfma_f32_16x16x32_bf16(a, b, c, 0, 0, 0);
}
DI s16x4 tr_read(const char* lds_p) {
  typedef __attribute__((ext_vector_type(4))) short v4i16_t;
  return __builtin_amdgcn_ds_read_tr16_b64_v4i16((LAS v4i16_t*)(lds_p));
}
DI bf16x8 tr_frag(const char* p0, const char* p1) {
  s16x4 lo = tr_read(p0), hi = tr_read(p1);
  return __builtin_shufflevector(lo, hi, 0, 1, 2, 3, 4, 5, 6, 7);
}
DI void glds16a(const void* gsrc, unsigned lds_dst) {
  unsigned keep;
  asm volatile("s_mov_b32 %0, m0\n\ts_mov_b32 m0, %2\n\ts_nop 0\n\tglobal_load_lds_dwordx4 %1, off\n\ts_mov_b32 m0, %0"
               : "=&s"(keep) : "v"(gsrc), "s"(lds_dst) : "memory");
}
DI void glds16a_sc1(const void* gsrc, unsigned lds_dst) {
  unsigned keep;
  asm volatile("s_mov_b32 %0, m0\n\ts_mov_b32 m0, %2\n\ts_nop 0\n\tglobal_load_lds_dwordx4 %1, off sc1\n\ts_mov_b32 m0, %0"
               : "=&s"(keep) : "v"(gsrc), "s"(lds_dst) : "memory");
}
DI void stats_acc(bf16x8 x, float& s, float& q) {
  u32x4 u = __builtin_bit_cast(u32x4, x);
  const bf2_t ones = {(__bf16)1.0f, (__bf16)1.0f};
#pragma unroll
  for (int i = 0; i < 4; ++i) {
    const unsigned w = u[i];
    bf2_t v = __builtin_bit_cast(bf2_t, w);
    s = __builtin_amdgcn_fdot2_f32_bf16(v, ones, s, false);
    q = __builtin_amdgcn_fdot2_f32_bf16(v, v, q, false);
  }
}
DI float gelu_tanh(float x) {
  const float a = -2.0f * 0.7978845608028654f * 1.4426950408889634f;
  const float b = a * 0.044715f;
  float t = x * __builtin_fmaf(x * x, b, a);
  float e = __builtin_amdgcn_exp2f(t);
  return x * __builtin_amdgcn_rcpf(1.0f + e);
}
#define WAIT_VM(n) asm volatile("s_waitcnt vmcnt(" #n ")" ::: "memory")
#define BAR() do { asm volatile("s_waitcnt lgkmcnt(0)" ::: "memory"); __builtin_amdgcn_s_barrier(); asm volatile("" ::: "memory"); } while (0)


typedef __amdgpu_buffer_rsrc_t rsrc_t;
DI bf16x8 ld16(rsrc_t rs, unsigned voff, unsigned soff) {
  return __builtin_bit_cast(bf16x8, __builtin_amdgcn_raw_buffer_load_b128(rs, voff, soff, 16));
}
DI void st16(rsrc_t rs, unsigned voff, u32x4 v) { __builtin_amdgcn_raw_buffer_store_b128(v, rs, voff, 0, 16); }
DI void st8(rsrc_t rs, unsigned voff, u32x2 v) { __builtin_amdgcn_raw_buffer_store_b64(v, rs, voff, 0, 16); }

template <int N>
DI void stage_w56(const u16* W, int ldw, int col0, int row0, unsigned lds_img, int first, int lane) {
  const int rin = lane >> 3, chp = lane & 7;
  const int sw0 = 2 * ((rin >> 1) & 1);
  int c0 = chp ^ sw0, c1 = chp ^ (sw0 | 4);
  const u16* src = W + (size_t)(row0 + first * 8 + rin) * ldw + col0;
  const u16* s0 = src + c0 * 8;
  const u16* s1 = src + c1 * 8;
#pragma unroll
  for (int i = 0; i < N; ++i) glds16a(((i & 1) ? s1 : s0) + (size_t)i * 8 * ldw, lds_img + (unsigned)(first + i) * 1024u);
}
DI void stage_img(const char* img, unsigned lds_img, int first, int lane) {
  const char* src = img + first * 1024 + lane * 16;
#pragma unroll
  for (int i = 0; i < 12; ++i) glds16a_sc1(src + i * 1024, lds_img + (unsigned)(first + i) * 1024u);
}
struct Dma56 {
  const u16* s0; int dlt; unsigned img; int ldw;
  DI void init(const u16* W, int ldw_, int col0, int row0, unsigned lds_img, int lane) {
    const int rin = lane >> 3, chp = lane & 7;
    const int sw0 = 2 * ((rin >> 1) & 1);
    s0 = W + (size_t)(row0 + rin) * ldw_ + col0 + (chp ^ sw0) * 8;
    dlt = ((chp ^ (sw0 | 4)) - (chp ^ sw0)) * 8; img = lds_img; ldw = ldw_;
  }
  DI void issue(int i) const { glds16a(s0 + (i * 8 * ldw + (i & 1) * dlt), img + (unsigned)i * 1024u); }
  DI void issue2(int rb, int dst) const { glds16a(s0 + (rb * 8 * ldw + (rb & 1) * dlt), img + (unsigned)dst * 1024u); }
};
struct NoHook { DI void operator()(int) const {} };
DI void stage_wqkv(const u16* wqkv, int h, unsigned lds_img, int g0, int lane) {
  const int colofs = 4 * (h & 1);
  int srcoff[3];
#pragma unroll
  for (int e = 0; e < 3; ++e) {
    int L = e * 64 + lane;
    int rr = L / 12, cc = L % 12;
    int seg = cc >> 2, c4 = cc & 3;
    int lc = c4 ^ (2 * ((rr >> 3) & 1));
    srcoff[e] = rr * 1344 + seg * 448 + (28 * h - colofs) + lc * 8;
  }
#pragma unroll
  for (int k = 0; k < 4; ++k) {
    const int grp = g0 + k;
    const u16* base = wqkv + (size_t)grp * 16 * 1344;
#pragma unroll
    for (int e = 0; e < 3; ++e) glds16a(base + srcoff[e], lds_img + (unsigned)(grp * 3 + e) * 1024u);
  }
}
DI float red_add(float x) {
  unsigned u = __float_as_uint(x);
  auto a = __builtin_amdgcn_permlane16_swap(u, u, false, false);
  x = __uint_as_float(a[0]) + __uint_as_float(a[1]);
  u = __float_as_uint(x);
  auto b = __builtin_amdgcn_permlane32_swap(u, u, false, false);
  return __uint_as_float(b[0]) + __uint_as_float(b[1]);
}
DI float red_max(float x) {
  unsigned u = __float_as_uint(x);
  auto a = __builtin_amdgcn_permlane16_swap(u, u, false, false);
  x = fmaxf(__uint_as_float(a[0]), __uint_as_float(a[1]));
  u = __float_as_uint(x);
  auto b = __builtin_amdgcn_permlane32_swap(u, u, false, false);
  return fmaxf(__uint_as_float(b[0]), __uint_as_float(b[1]));
}
DI float red_min(float x) {
  unsigned u = __float_as_uint(x);
  auto a = __builtin_amdgcn_permlane16_swap(u, u, false, false);
  x = fminf(__uint_as_float(a[0]), __uint_as_float(a[1]));
  u = __float_as_uint(x);
  auto b = __builtin_amdgcn_permlane32_swap(u, u, false, false);
  return fminf(__uint_as_float(b[0]), __uint_as_float(b[1]));
}
constexpr unsigned OOB = 0xfffffff0u;
constexpr unsigned SPIN_CAP = 1u << 20;
DI u32x4 ld16u(rsrc_t rs, unsigned voff) { return __builtin_amdgcn_raw_buffer_load_b128(rs, voff, 0, 16); }
DI void put_stats(rsrc_t rs, unsigned off, u32x2 a, u32x2 b, int g) {
  float v[8] = {bflo(a[0]), bfhi(a[0]), bflo(a[1]), bfhi(a[1]), bflo(b[0]), bfhi(b[0]), bflo(b[1]), bfhi(b[1])};
  float s = ((v[0] + v[1]) + (v[2] + v[3])) + ((v[4] + v[5]) + (v[6] + v[7]));
  float q = (v[0] * v[0] + v[1] * v[1] + v[2] * v[2] + v[3] * v[3]) + (v[4] * v[4] + v[5] * v[5] + v[6] * v[6] + v[7] * v[7]);
  s = red_add(s); q = red_add(q);
  if (g == 0) { u32x2 o; o[0] = __float_as_uint(s); o[1] = __float_as_uint(q); st8(rs, off, o); }
}
DI void get_stats(u32x4 a, u32x4 b, float& s, float& q) {
  s = (__uint_as_float(a[0]) + __uint_as_float(a[2])) + (__uint_as_float(b[0]) + __uint_as_float(b[2]));
  q = (__uint_as_float(a[1]) + __uint_as_float(a[3])) + (__uint_as_float(b[1]) + __uint_as_float(b[3]));
  s = red_add(s); q = red_add(q);
}

template <int NT, int KS, int ROWB, int XN, class HOOK = NoHook>
DI void gemm_loop(const char* Wimg, const int (&toff)[NT], const bf16x8 (&xf)[XN], f32x4 (&acc)[NT], HOOK hook = HOOK()) {
  bf16x8 wa[NT], wb[NT];
#pragma unroll
  for (int t = 0; t < NT; ++t) wa[t] = tr_frag(Wimg + toff[t], Wimg + toff[t] + 4 * ROWB);
  __builtin_amdgcn_sched_barrier(0);
#pragma unroll
  for (int s = 0; s < KS; s += 2) {
    if (s + 1 < KS) {
#pragma unroll
      for (int t = 0; t < NT; ++t) wb[t] = tr_frag(Wimg + toff[t] + (s + 1) * 32 * ROWB, Wimg + toff[t] + (s + 1) * 32 * ROWB + 4 * ROWB);
    }
#pragma unroll
    for (int t = 0; t < NT; ++t) acc[t] = mfma16(wa[t], xf[s], acc[t]);
    if (s + 1 < KS) {
#pragma unroll
      for (int t = 0; t < NT; ++t) { __builtin_amdgcn_sched_group_barrier(0x8, 1, 0); __builtin_amdgcn_sched_group_barrier(0x100, 2, 0); }
    }
    __builtin_amdgcn_sched_barrier(0);
    hook(s);
    __builtin_amdgcn_sched_barrier(0);
    if (s + 1 < KS) {
      if (s + 2 < KS) {
#pragma unroll
        for (int t = 0; t < NT; ++t) wa[t] = tr_frag(Wimg + toff[t] + (s + 2) * 32 * ROWB, Wimg + toff[t] + (s + 2) * 32 * ROWB + 4 * ROWB);
      }
#pragma unroll
      for (int t = 0; t < NT; ++t) acc[t] = mfma16(wb[t], xf[s + 1], acc[t]);
      if (s + 2 < KS) {
#pragma unroll
        for (int t = 0; t < NT; ++t) { __builtin_amdgcn_sched_group_barrier(0x8, 1, 0); __builtin_amdgcn_sched_group_barrier(0x100, 2, 0); }
      }
      __builtin_amdgcn_sched_barrier(0);
      hook(s + 1);
      __builtin_amdgcn_sched_barrier(0);
    }
  }
}
template <int NT, int KS, int ROWB, class HOOK = NoHook>
DI void gemm_loop2(const char* Wimg, const int (&toff)[NT], const bf16x8 (&xa)[KS], const bf16x8 (&xb)[KS], f32x4 (&aa)[NT], f32x4 (&ab)[NT], HOOK hook = HOOK()) {
  bf16x8 wa[NT], wb[NT];
#pragma unroll
  for (int t = 0; t < NT; ++t) wa[t] = tr_frag(Wimg + toff[t], Wimg + toff[t] + 4 * ROWB);
  __builtin_amdgcn_sched_barrier(0);
#pragma unroll
  for (int s = 0; s < KS; s += 2) {
    if (s + 1 < KS) {
#pragma unroll
      for (int t = 0; t < NT; ++t) wb[t] = tr_frag(Wimg + toff[t] + (s + 1) * 32 * ROWB, Wimg + toff[t] + (s + 1) * 32 * ROWB + 4 * ROWB);
    }
#pragma unroll
    for (int t = 0; t < NT; ++t) { aa[t] = mfma16(wa[t], xa[s], aa[t]); ab[t] = mfma16(wa[t], xb[s], ab[t]); }
    if (s + 1 < KS) {
#pragma unroll
      for (int t = 0; t < NT; ++t) { __builtin_amdgcn_sched_group_barrier(0x8, 2, 0); __builtin_amdgcn_sched_group_barrier(0x100, 2, 0); }
    }
    __builtin_amdgcn_sched_barrier(0);
    hook(s);
    __builtin_amdgcn_sched_barrier(0);
    if (s + 1 < KS) {
      if (s + 2 < KS) {
#pragma unroll
        for (int t = 0; t < NT; ++t) wa[t] = tr_frag(Wimg + toff[t] + (s + 2) * 32 * ROWB, Wimg + toff[t] + (s + 2) * 32 * ROWB + 4 * ROWB);
      }
#pragma unroll
      for (int t = 0; t < NT; ++t) { aa[t] = mfma16(wb[t], xa[s + 1], aa[t]); ab[t] = mfma16(wb[t], xb[s + 1], ab[t]); }
      if (s + 2 < KS) {
#pragma unroll
        for (int t = 0; t < NT; ++t) { __builtin_amdgcn_sched_group_barrier(0x8, 2, 0); __builtin_amdgcn_sched_group_barrier(0x100, 2, 0); }
      }
      __builtin_amdgcn_sched_barrier(0);
      hook(s + 1);
      __builtin_amdgcn_sched_barrier(0);
    }
  }
}
template <int KS, int ROWB>
DI f32x4 colsum_tile(const char* Wp, bf16x8 ones) {
  bf16x8 w[KS];
#pragma unroll
  for (int s = 0; s < KS; ++s) w[s] = tr_frag(Wp + s * 32 * ROWB, Wp + s * 32 * ROWB + 4 * ROWB);
  __builtin_amdgcn_sched_barrier(0);
  f32x4 c0 = {0.f, 0.f, 0.f, 0.f}, c1 = c0, c2 = c0;
#pragma unroll
  for (int s = 0; s < KS; ++s) { if (s % 3 == 0) c0 = mfma16(w[s], ones, c0); else if (s % 3 == 1) c1 = mfma16(w[s], ones, c1); else c2 = mfma16(w[s], ones, c2); }
  return (c0 + c1) + c2;
}

struct Args {
  const u16 *x0, *wqkv, *wphi, *wo, *w1, *w2, *w3, *w4;
  u16* out;
  char* ws;
};

template <int SKIP, bool DRAIN = true>
DI void wait_flags(gu64* base, int n, int stride, u64 tag, int wv, int lane, gu64* extra = nullptr) {
  if (wv == 0) {
    bool use = lane < n && !(SKIP == 1 && (lane & 7) == 7) && !(SKIP == 2 && (lane & 15) >= 14);
    gu64* ptr = base + lane * stride;
    if (extra != nullptr && lane == 32) { use = true; ptr = extra; }
    for (unsigned spins = 0; spins < SPIN_CAP; ++spins) {
      u64 v = tag;
      if (use) v = __hip_atomic_load(ptr, RLX_AGENT);
      if (__all(v == tag)) break;
      __builtin_amdgcn_s_sleep(1);
    }
  }
  if (DRAIN) WAIT_VM(0);
  BAR();
}
DI void publish(gu64* flag, u64 tag) {
  WAIT_VM(0);
  BAR();
  if (threadIdx.x == 0) __hip_atomic_store(flag, tag, RLX_AGENT);
}
DI u32x4 pair16(u32x2 a, u32x2 b) {
  auto sx = __builtin_amdgcn_permlane16_swap(a[0], b[0], false, false);
  auto sy = __builtin_amdgcn_permlane16_swap(a[1], b[1], false, false);
  u32x4 r; r[0] = sx[0]; r[1] = sy[0]; r[2] = sx[1]; r[3] = sy[1];
  return r;
}

#define W4CHUNK(pk, i) ((pk) == 0 ? ((i) < 4 ? 2 * (i) : 2 * ((i) - 4) + 1) : ((i) < 3 ? 2 * (i) + 1 : 2 * ((i) - 3)))
DI int launder(int x) { asm volatile("" : "+v"(x)); return x; }
DI int launder_s(int x) { asm volatile("" : "+s"(x)); return x; }
#define LANEVARS \
  const int wv = launder_s(wv0), id = launder_s(id0); \
  const int panel = id >> 4, h = id & 15, m = id >> 3, j = id & 7, tg = wv & 3, kh = wv >> 2; \
  const bool act64 = j < 7, act6 = h < 14; \
  (void)panel; (void)h; (void)m; (void)j; (void)tg; (void)kh; (void)act64; (void)act6; \
  const int lane = launder(lane0); \
  const int tok = lane & 15, g = lane >> 4, q = (lane & 15) >> 2, p = lane & 3; \
  const bool padlane = (g == 3); \
  const int tid = wv * 64 + lane; \
  const int trow64 = m * 64 + tg * 16 + tok; \
  const int trow128 = panel * 128 + wv * 16 + tok; \
  const int swA = 2 * (((q >> 1) & 1) | ((g & 1) << 1)); \
  const int colu = 64 * j + 32 * kh + 16 * (g & 1) + 8 * (g >> 1); \
  const unsigned st_frag = (unsigned)((((trow64 >> 4) * 14 + (colu >> 5)) * 64 + ((colu >> 3) & 3) * 16 + tok) * 16); \
  (void)tok; (void)g; (void)q; (void)p; (void)padlane; (void)tid; (void)trow64; (void)trow128; (void)swA; (void)colu; (void)st_frag;

__global__ void __launch_bounds__(512, 2) mega(Args a) {
  __shared__ __attribute__((aligned(16))) char lds[163840];
  const unsigned ldsb = (unsigned)(uintptr_t)(LAS char*)lds;
  char* const scr = lds + 131072;

  const int lane0 = threadIdx.x & 63;
  const int wv = __builtin_amdgcn_readfirstlane(threadIdx.x >> 6);
  const int id = (blockIdx.x & 7) * 32 + (blockIdx.x >> 3);
  const int wv0 = wv, id0 = id;
  const int panel = id >> 4, h = id & 15;
  const int m = id >> 3, j = id & 7;
  const int tg = wv & 3, kh = wv >> 2;
  const bool act64 = j < 7, act6 = h < 14;

  if (wv >= 4) __builtin_amdgcn_s_setprio(1);
  const rsrc_t rs_ws = __builtin_amdgcn_make_buffer_rsrc((void*)a.ws, 0, (int)WS_USED, 0x00020000);
  const rsrc_t rs_x0 = __builtin_amdgcn_make_buffer_rsrc((void*)a.x0, 0, (int)ACT, 0x00020000);
  const rsrc_t rs_out = __builtin_amdgcn_make_buffer_rsrc((void*)a.out, 0, (int)ACT, 0x00020000);
  gu64* const flags = (gu64*)(a.ws + OFF_FLAGS);

  if (wv) stage_wqkv(a.wqkv, h, ldsb, 4 * (wv - 1), lane0);

  u32x2 xres[2], x2r[2], xnr[2], x1r[2];
  bf16x8 wpf[2];
  u64 E = 0;
  bf16x8 ones;
#pragma unroll
  for (int jj = 0; jj < 8; ++jj) ones[jj] = (short)0x3f80;

  for (int pair = 0; pair < 2; ++pair) {
    const unsigned pb = OFF_ACT + pair * PAIR_BYTES;
    const unsigned o_off = pb, x2_off = pb + ACT, h_off = pb + 2 * ACT, x1_off = pb + 3 * ACT, xp_off = pb + 4 * ACT, h3_off = pb + 5 * ACT;
    const unsigned sx2_off = pb + 9 * ACT, sx1_off = sx2_off + STB, sxp_off = sx1_off + STB;
    gu64* const fl = flags + pair * 8 * 256;

    bf16x8 pqf[2];
    {
      LANEVARS
      char* Wimg = lds;
      char* Pimg = scr;
      char* Vimg = scr + 16384;
      float* cs = (float*)(scr + 24576 + 6656);
      const int colofs = 4 * (h & 1);
      const int kk = wv & 1;
      int toff[6];
#pragma unroll
      for (int t = 0; t < 6; ++t) {
        int bb = (t >> 1) * 64 + (colofs + 16 * (t & 1) + 4 * p) * 2;
        int ph = ((bb >> 4) ^ (2 * (g & 1))) * 16 + (bb & 15);
        toff[t] = (kk * 224 + 8 * g + q) * 192 + ph;
      }
      u32x4 sa = {0u, 0u, 0u, 0u}, sb = sa;
      bf16x8 xa[7], xb[7];
      unsigned warm0 = 0, warm1 = 0;
      auto do_colsum = [&]() {
        if (wv < 6) {
          int bb = (wv >> 1) * 64 + (colofs + 16 * (wv & 1) + 4 * p) * 2;
          int ph = ((bb >> 4) ^ (2 * (g & 1))) * 16 + (bb & 15);
          int o = (8 * g + q) * 192 + ph;
          f32x4 c = colsum_tile<14, 192>(Wimg + o, ones);
          if (tok == 0) *(f32x4*)(cs + 16 * wv + 4 * g) = c;
        }
      };
      if (pair == 0) {
        const unsigned b0 = (unsigned)trow128 * 896u + kk * 448 + g * 16;
        const unsigned b1 = (unsigned)(trow128 ^ 16) * 896u + kk * 448 + g * 16;
#pragma unroll
        for (int s = 0; s < 7; ++s) { xa[s] = ld16(rs_x0, b0 + 64 * s, 0); xb[s] = ld16(rs_x0, b1 + 64 * s, 0); }
        __builtin_amdgcn_sched_barrier(0);
        E = __hip_atomic_load((gu64*)a.ws, RLX_AGENT);
#pragma unroll
        for (int tt = 0; tt < 2; ++tt) {
          const int cl = 32 * kh + 16 * tt + 4 * g;
          xres[tt] = *(const u32x2*)(a.x0 + (size_t)trow64 * D_ + (act64 ? 64 * j : 0) + cl);
        }
        if (wv == 0) {
          const u32x4 w0 = *(const u32x4*)(a.wphi + h * 784 + lane * 8);
          const u32x4 w1v = *(const u32x4*)(a.wphi + h * 784 + (lane < 34 ? 512 + lane * 8 : 0));
          *(u32x4*)(scr + 24576 + lane * 16) = w0;
          if (lane < 34) *(u32x4*)(scr + 24576 + 1024 + lane * 16) = w1v;
        }
#pragma unroll
        for (int s = 0; s < 7; ++s) { asm volatile("" : "+v"(xa[s])); asm volatile("" : "+v"(xb[s])); }
        asm volatile("" : "+v"(xres[0]), "+v"(xres[1]), "+v"(E));
        WAIT_VM(0);
        BAR();
        __builtin_amdgcn_sched_barrier(0);
        {
          const u16* wsel = wv == 0 ? a.wo : wv == 1 ? a.w1 : wv == 2 ? a.w2 : wv == 3 ? a.w3 : a.w4;
          const rsrc_t rw = __builtin_amdgcn_make_buffer_rsrc((void*)wsel, 0, wv < 3 ? D_ * D_ * 2 : D_ * F_ * 2, 0x00020000);
          const unsigned ow = wv < 3 ? (lane < 13 ? (unsigned)id * 1568u + lane * 128u : OOB) : (wv < 5 && lane < 49 ? (unsigned)id * 6272u + lane * 128u : OOB);
          unsigned os = OOB;
          if (wv == 3) os = lane < 54 ? (unsigned)id * 6912u + lane * 128u : OOB;
          else if (wv == 4 || wv == 5) os = lane < 56 ? OFF_ACT + (wv - 4) * PAIR_BYTES + (unsigned)id * 7168u + lane * 128u : OOB;
          else if (wv >= 6) os = lane < 24 ? OFF_ACT + (wv - 6) * PAIR_BYTES + 9 * ACT + (unsigned)id * 3072u + lane * 128u : OOB;
          asm volatile("s_nop 4\n\tbuffer_load_dword %0, %2, %3, 0 offen sc1\n\tbuffer_load_dword %1, %4, %5, 0 offen"
                       : "=&v"(warm0), "=&v"(warm1) : "v"(os), "s"(rs_ws), "v"(ow), "s"(rw) : "memory");
        }
        __builtin_amdgcn_sched_barrier(0);
        do_colsum();
        {
          const u16* wl = (const u16*)(scr + 24576);
#pragma unroll
          for (int t = 0; t < 2; ++t)
#pragma unroll
            for (int jj = 0; jj < 8; ++jj) {
              int d = 16 * (jj >> 2) + 4 * g + (jj & 3), f = 16 * t + tok;
              bool ok = (d < 28) && (f < 28);
              const u16 v = wl[ok ? d * 28 + f : 0];
              wpf[t][jj] = ok ? (short)v : (short)0;
            }
        }
      } else {
        do_colsum();
        const unsigned ba = OFF_ACT + 4 * ACT + (unsigned)((trow128 >> 4) * 14 + kk * 7) * 1024u + lane * 16;
        const unsigned bb2 = OFF_ACT + 4 * ACT + (unsigned)(((trow128 >> 4) ^ 1) * 14 + kk * 7) * 1024u + lane * 16;
        const unsigned so = OFF_ACT + 9 * ACT + 2 * STB + (unsigned)(trow128 >> 4) * 1792u + g * 256 + tok * 16;
        const unsigned so2 = padlane ? OOB : so + 1024;
        asm volatile("" :: "v"(ba), "v"(bb2), "v"(so), "v"(so2));
        wait_flags<1>(flags + 6 * 256 + panel * 16, 16, 1, E + 1 + 6, wv, lane);
#pragma unroll
        for (int s = 0; s < 7; ++s) { xa[s] = ld16(rs_ws, ba + 1024 * s, 0); xb[s] = ld16(rs_ws, bb2 + 1024 * s, 0); }
        sa = ld16u(rs_ws, so); sb = ld16u(rs_ws, so2);
      }
      auto phi = [&](u32x4 u, u32x4& op, u32x4& om) {
        bf16x8 qf = __builtin_bit_cast(bf16x8, u);
        f32x4 pz = {0.f, 0.f, 0.f, 0.f};
        f32x4 p0 = mfma16(wpf[0], qf, pz);
        f32x4 p1 = mfma16(wpf[1], qf, pz);
        float pv[8];
#pragma unroll
        for (int r = 0; r < 4; ++r) { pv[r] = p0[r]; pv[4 + r] = padlane ? p0[r] : p1[r]; }
        float mx = fmaxf(fmaxf(fmaxf(pv[0], pv[1]), fmaxf(pv[2], pv[3])), fmaxf(fmaxf(pv[4], pv[5]), fmaxf(pv[6], pv[7])));
        float mn = fminf(fminf(fminf(pv[0], pv[1]), fminf(pv[2], pv[3])), fminf(fminf(pv[4], pv[5]), fminf(pv[6], pv[7])));
        mx = red_max(mx); mn = red_min(mn);
        float ep[8], em[8], sp = 0.f, sm = 0.f;
        const float L2E = 1.4426950408889634f;
        const float mxl = mx * L2E, mnl = mn * L2E;
#pragma unroll
        for (int i = 0; i < 8; ++i) {
          ep[i] = __builtin_amdgcn_exp2f(__builtin_fmaf(pv[i], L2E, -mxl));
          em[i] = __builtin_amdgcn_exp2f(__builtin_fmaf(pv[i], -L2E, mnl));
          if (i >= 4 && padlane) { ep[i] = 0.f; em[i] = 0.f; }
          sp += ep[i]; sm += em[i];
        }
        sp = red_add(sp); sm = red_add(sm);
        const float ip = __builtin_amdgcn_rcpf(sp), im = __builtin_amdgcn_rcpf(sm);
#pragma unroll
        for (int i = 0; i < 4; ++i) {
          op[i] = pack2(ep[2 * i] * ip, ep[2 * i + 1] * ip);
          om[i] = pack2(em[2 * i] * im, em[2 * i + 1] * im);
        }
      };
      float rstd, mr;
      const int tl = wv * 16 + tok;
      {
        const int tkv[4] = {toff[2], toff[3], toff[4], toff[5]};
        f32x4 acc[4], acb[4];
#pragma unroll
        for (int t = 0; t < 4; ++t) { acc[t] = (f32x4){0.f, 0.f, 0.f, 0.f}; acb[t] = acc[t]; }
        float ssum = 0.f, ssq = 0.f, bsum = 0.f, bsq = 0.f;
        gemm_loop2<4, 7, 192>(Wimg, tkv, xa, xb, acc, acb);
        if (pair) {
          get_stats(sa, sb, ssum, ssq);
        } else {
#pragma unroll
          for (int s = 0; s < 7; ++s) { stats_acc(xa[s], ssum, ssq); stats_acc(xb[s], bsum, bsq); }
          ssum = red_add(ssum); ssq = red_add(ssq); bsum = red_add(bsum); bsq = red_add(bsq);
        }
        {
          f32x4* mine = (f32x4*)(lds + 86016 + 64 + wv * 4608);
#pragma unroll
          for (int t = 0; t < 4; ++t) mine[t * 64 + lane] = acb[t];
          *(f2_t*)(lds + 86016 + 64 + wv * 4608 + 4096 + lane * 8) = (f2_t){bsum, bsq};
        }
        BAR();
        {
          const f32x4* other = (const f32x4*)(lds + 86016 + 64 + (wv ^ 1) * 4608);
#pragma unroll
          for (int t = 0; t < 4; ++t) acc[t] += other[t * 64 + lane];
          f2_t o = *(const f2_t*)(lds + 86016 + 64 + (wv ^ 1) * 4608 + 4096 + lane * 8);
          ssum += o[0]; ssq += o[1];
        }
        const float mean = ssum * (1.0f / D_);
        const float var = fmaxf(ssq * (1.0f / D_) - mean * mean, 0.f);
        rstd = __builtin_amdgcn_rsqf(var + 1e-5f);
        mr = -mean * rstd;
        float val[4][4];
#pragma unroll
        for (int t = 0; t < 4; ++t) {
          f32x4 c = *(const f32x4*)(cs + 16 * (2 + t) + 4 * g);
#pragma unroll
          for (int r = 0; r < 4; ++r) val[t][r] = __builtin_fmaf(acc[t][r], rstd, mr * c[r]);
        }
        u32x4 u, op, om;
        u[0] = pack2(val[0][0], val[0][1]); u[1] = pack2(val[0][2], val[0][3]);
        u[2] = pack2(val[1][0], val[1][1]); u[3] = pack2(val[1][2], val[1][3]);
        phi(u, op, om);
        const int sw = 2 * (((tl >> 1) & 1) | (((tl >> 3) & 1) << 1));
#pragma unroll
        for (int part = 0; part < 2; ++part)
#pragma unroll
          for (int t = 0; t < 2; ++t)
            if (t == 0 || !padlane) {
              int bb = (part * 28 + 16 * t + 4 * g) * 2;
              u32x2 v; v[0] = part ? om[2 * t] : op[2 * t]; v[1] = part ? om[2 * t + 1] : op[2 * t + 1];
              *(u32x2*)(Pimg + tl * 128 + (((bb >> 4) ^ sw) << 4) + (bb & 15)) = v;
            }
        const int swv = 2 * ((tl >> 3) & 1);
#pragma unroll
        for (int t = 0; t < 2; ++t) {
          u32x2 o;
          o[0] = pack2(val[2 + t][0], val[2 + t][1]);
          o[1] = pack2(val[2 + t][2], val[2 + t][3]);
          if (t == 1 && padlane) { o[0] = 0x00003f80u; o[1] = 0u; }
          int bb = (16 * t + 4 * g) * 2;
          *(u32x2*)(Vimg + tl * 64 + (((bb >> 4) ^ swv) << 4) + (bb & 15)) = o;
        }
        BAR();
        {
          const int td = wv & 1, tf = wv >> 1;
          const int bbv = (16 * td + 4 * p) * 2;
          const int vo = (8 * g + q) * 64 + (((bbv >> 4) ^ (2 * (g & 1))) << 4) + (bbv & 15);
          const int bbp = (16 * tf + 4 * p) * 2;
          const int po = (8 * g + q) * 128 + (((bbp >> 4) ^ swA) << 4) + (bbp & 15);
          bf16x8 vf[4], pf[4];
#pragma unroll
          for (int s = 0; s < 4; ++s) {
            vf[s] = tr_frag(Vimg + vo + s * 32 * 64, Vimg + vo + s * 32 * 64 + 4 * 64);
            pf[s] = tr_frag(Pimg + po + s * 32 * 128, Pimg + po + s * 32 * 128 + 4 * 128);
          }
          __builtin_amdgcn_sched_barrier(0);
          f32x4 c = {0.f, 0.f, 0.f, 0.f};
#pragma unroll
          for (int s = 0; s < 4; ++s) c = mfma16(pf[s], vf[s], c);
          const int d = 16 * td + tok, f = 16 * tf + 4 * g;
          if (d < 29 && f < 56) {
            u32x2 o; o[0] = pack2(c[0], c[1]); o[1] = pack2(c[2], c[3]);
            st8(rs_ws, OFF_KVP + pair * KVP_PAIR + (unsigned)id * (KVSZ * 2) + (unsigned)(d * 56 + f) * 2u, o);
          }
        }
      }
      publish(fl + h * 16 + panel, E + 1 + pair * 8);
      asm volatile("" :: "v"(warm0), "v"(warm1));
      if (pair == 0 && panel == h) {
#pragma unroll
        for (int i = 0; i < 11; ++i) {
          const int pc = wv * 11 + i;
          if (pc < 84) st16(rs_ws, OFF_IMG + (unsigned)h * 86016u + pc * 1024 + lane * 16, *(const u32x4*)(lds + pc * 1024 + lane * 16));
        }
      }
      {
        const int tq[2] = {toff[0], toff[1]};
        f32x4 acc[2], acb[2];
#pragma unroll
        for (int t = 0; t < 2; ++t) { acc[t] = (f32x4){0.f, 0.f, 0.f, 0.f}; acb[t] = acc[t]; }
        Dma56 dm; dm.init(a.wo, D_, 64 * j, 0, ldsb + 73728, lane);
        gemm_loop2<2, 7, 192>(Wimg, tq, xa, xb, acc, acb, [&](int s) {
          if (s < 6) { const int i = 14 + 6 * wv + s; if (act64 && i < 56) dm.issue(i); }
        });
        {
          f32x4* mine = (f32x4*)(scr + wv * 2048);
          mine[lane] = acb[0]; mine[64 + lane] = acb[1];
        }
        const int t256 = tid & 255;
        const unsigned kvo = (t256 < 203) ? OFF_KVP + pair * KVP_PAIR + (unsigned)(kh * 128 + h) * (KVSZ * 2) + t256 * 16 : OOB;
        wait_flags<0, false>(fl + h * 16, 16, 1, E + 1 + pair * 8, wv, lane);
        u32x4 v[8];
#pragma unroll
        for (int r = 0; r < 8; ++r) v[r] = __builtin_amdgcn_raw_buffer_load_b128(rs_ws, kvo, r * 16 * (KVSZ * 2), 16);
        __builtin_amdgcn_sched_barrier(0);
        {
          const f32x4* other = (const f32x4*)(scr + (wv ^ 1) * 2048);
          acc[0] += other[lane]; acc[1] += other[64 + lane];
        }
        if (wv >= 4 && act64) stage_w56<4>(a.wo, D_, 64 * j, 0, ldsb + 73728, 4 * (wv - 4), lane);
        float val[2][4];
#pragma unroll
        for (int t = 0; t < 2; ++t) {
          f32x4 c = *(const f32x4*)(cs + 16 * t + 4 * g);
#pragma unroll
          for (int r = 0; r < 4; ++r) val[t][r] = __builtin_fmaf(acc[t][r], rstd, mr * c[r]);
        }
        u32x4 u, op, om;
        u[0] = pack2(val[0][0], val[0][1]); u[1] = pack2(val[0][2], val[0][3]);
        u[2] = pack2(val[1][0], val[1][1]); u[3] = pack2(val[1][2], val[1][3]);
        phi(u, op, om);
        pqf[0] = __builtin_bit_cast(bf16x8, op);
        pqf[1] = __builtin_bit_cast(bf16x8, om);
        __builtin_amdgcn_sched_barrier(0);
        char* KVw = scr + 16384;
        float sum[8];
#pragma unroll
        for (int i = 0; i < 4; ++i) { sum[2 * i] = bflo(v[0][i]); sum[2 * i + 1] = bfhi(v[0][i]); }
#pragma unroll
        for (int r = 1; r < 8; ++r)
#pragma unroll
          for (int i = 0; i < 4; ++i) { sum[2 * i] += bflo(v[r][i]); sum[2 * i + 1] += bfhi(v[r][i]); }
        f32x4* hs = (f32x4*)(scr + 21504);
        if (kh) { hs[t256 * 2] = (f32x4){sum[0], sum[1], sum[2], sum[3]}; hs[t256 * 2 + 1] = (f32x4){sum[4], sum[5], sum[6], sum[7]}; }
        if (wv == 4) *(u32x2*)(KVw + (lane >> 1) * 136 + 112 + (lane & 1) * 8) = (u32x2){0u, 0u};
        BAR();
        if (tid < 203) {
          const f32x4 h0 = hs[tid * 2], h1 = hs[tid * 2 + 1];
          const int d = tid / 7, c8 = tid % 7;
          u32x2 o0, o1;
          o0[0] = pack2(sum[0] + h0[0], sum[1] + h0[1]); o0[1] = pack2(sum[2] + h0[2], sum[3] + h0[3]);
          o1[0] = pack2(sum[4] + h1[0], sum[5] + h1[1]); o1[1] = pack2(sum[6] + h1[2], sum[7] + h1[3]);
          *(u32x2*)(KVw + d * 136 + c8 * 16) = o0;
          *(u32x2*)(KVw + d * 136 + c8 * 16 + 8) = o1;
        }
      }
    }
    const u64 tag0 = E + 1 + pair * 8;
    {
      LANEVARS
      char* KVimg = scr + 16384;
      BAR();
      f32x4 nd[2];
#pragma unroll
      for (int td = 0; td < 2; ++td) {
        nd[td] = (f32x4){0.f, 0.f, 0.f, 0.f};
        const char* rowp = KVimg + (16 * td + tok) * 136;
#pragma unroll
        for (int s = 0; s < 2; ++s) {
          u32x2 lo = *(const u32x2*)(rowp + (28 * s + 4 * g) * 2);
          u32x2 hi = *(const u32x2*)(rowp + (28 * s + 16 + 4 * g) * 2);
          u32x4 u; u[0] = lo[0]; u[1] = lo[1]; u[2] = hi[0]; u[3] = hi[1];
          nd[td] = mfma16(__builtin_bit_cast(bf16x8, u), pqf[s], nd[td]);
        }
      }
      const float den = red_add(padlane ? nd[1][0] : 0.f) + 1e-6f;
      const float inv = 1.0f / den;
#pragma unroll
      for (int td = 0; td < 2; ++td) {
        if (td == 0 || !padlane) {
          u32x2 o;
          o[0] = pack2(nd[td][0] * inv, nd[td][1] * inv);
          o[1] = pack2(nd[td][2] * inv, nd[td][3] * inv);
          const int col = 28 * h + 16 * td + 4 * g;
          const unsigned off = o_off + (unsigned)((((trow128 >> 4) * 14 + (col >> 5)) * 64 + ((col >> 3) & 3) * 16 + tok) * 16 + (col & 7) * 2);
          st8(rs_ws, off, o);
        }
      }
      publish(fl + 1 * 256 + id, tag0 + 1);
      if (pair == 0 && panel == h && threadIdx.x == 0) __hip_atomic_store(flags + 16 * 256 + h * 16, tag0 + 5, RLX_AGENT);
      if (wv) {
        if (act64) stage_w56<8>(a.w1, D_, 64 * j, 0, ldsb, 8 * (wv - 1), lane);
        else if (act6) {
          stage_w56<8>(a.w3, F_, 128 * h, 0, ldsb, 8 * (wv - 1), lane);
          stage_w56<8>(a.w3, F_, 128 * h + 64, 0, ldsb + 73728, 8 * (wv - 1), lane);
        }
      }
    }
    float* const xch = (float*)scr;
    float* const cs2 = xch + 4096;
    if (act64) {
#pragma unroll
    for (int ph = 0; ph < 3; ++ph) {
      LANEVARS
      const char* Wimg = lds + (ph == 0 ? 73728 : ph == 1 ? 0 : 57344);
      int toff[4];
#pragma unroll
      for (int t = 0; t < 4; ++t) toff[t] = (kh * 224 + 8 * g + q) * 128 + (((2 * t + (p >> 1)) ^ swA) << 4) + (p & 1) * 8;
      unsigned xo[7];
      unsigned so = 0, so2 = 0;
      if (ph < 2) {
        xo[0] = (ph == 0 ? o_off : x2_off) + (unsigned)((trow64 >> 4) * 14 + kh * 7) * 1024u + lane * 16;
        asm volatile("" : "+v"(xo[0]));
        if (ph == 1) { so = sx2_off + (unsigned)(trow64 >> 4) * 1792u + g * 256 + tok * 16; so2 = padlane ? OOB : so + 1024; asm volatile("" : "+v"(so), "+v"(so2)); }
      } else {
#pragma unroll
        for (int s = 0; s < 7; ++s) {
          const int k = kh * 224 + 32 * s + 8 * g;
          const int grp = k / 112;
          const int sh = (grp == 0) ? -1 : (grp == 1) ? 1 : (grp == 2) ? -2 : 2;
          const int sr = trow64 + sh;
          const bool ok = (sr >= 0) && (sr < S_);
          xo[s] = ok ? h_off + (unsigned)((((sr >> 4) * 14 + kh * 7 + s) * 64 + g * 16 + (sr & 15)) * 16) : OOB;
          asm volatile("" : "+v"(xo[s]));
        }
      }
#pragma unroll
      for (int t = 0; t < 4; ++t) asm volatile("" : "+v"(toff[t]));
      if (ph == 1) {
        const int o = (kh * 224 + 8 * g + q) * 128 + (((2 * tg + (p >> 1)) ^ swA) << 4) + (p & 1) * 8;
        f32x4 c = colsum_tile<7, 128>(Wimg + o, ones);
        if (tok == 0) *(f32x4*)(cs2 + kh * 64 + 16 * tg + 4 * g) = c;
      }
      if (ph == 0) wait_flags<0, false>(fl + 1 * 256 + panel * 16, 16, 1, tag0 + 1, wv, lane);
      else if (ph == 1) wait_flags<1, false>(fl + 2 * 256 + m * 8, 8, 1, tag0 + 2, wv, lane);
      else {
        const int lo = m > 0 ? m - 1 : 0, hi = m < 31 ? m + 1 : 31;
        wait_flags<1, false>(fl + 3 * 256 + lo * 8, (hi - lo + 1) * 8, 1, tag0 + 3, wv, lane);
      }
      bf16x8 xf[7];
      u32x4 sa = {0u, 0u, 0u, 0u}, sb = sa;
#pragma unroll
      for (int s = 0; s < 7; ++s) xf[s] = ld16(rs_ws, ph < 2 ? xo[0] + 1024 * s : xo[s], 0);
      if (ph == 1) { sa = ld16u(rs_ws, so); sb = ld16u(rs_ws, so2); }
      f32x4 acc[4];
#pragma unroll
      for (int t = 0; t < 4; ++t) acc[t] = (f32x4){0.f, 0.f, 0.f, 0.f};
      float ssum = 0.f, ssq = 0.f;
      gemm_loop<4, 7, 128>(Wimg, toff, xf, acc);
      {
        f32x4* mine = (f32x4*)(xch + wv * 512);
        if (kh) { mine[lane] = acc[0]; mine[64 + lane] = acc[1]; acc[0] = acc[2]; acc[1] = acc[3]; }
        else { mine[lane] = acc[2]; mine[64 + lane] = acc[3]; }
      }
      if (ph == 1) get_stats(sa, sb, ssum, ssq);
      BAR();
      {
        const f32x4* other = (const f32x4*)(xch + (wv ^ 4) * 512);
        acc[0] += other[lane];
        acc[1] += other[64 + lane];
        u32x2 res[2];
        if (ph == 0) {
#pragma unroll
          for (int tt = 0; tt < 2; ++tt) {
            res[tt][0] = pack2(bflo(xres[tt][0]) + acc[tt][0], bfhi(xres[tt][0]) + acc[tt][1]);
            res[tt][1] = pack2(bflo(xres[tt][1]) + acc[tt][2], bfhi(xres[tt][1]) + acc[tt][3]);
            x2r[tt] = res[tt]; asm volatile("" : "+v"(x2r[tt]));
          }
        } else if (ph == 1) {
          const float mean = ssum * (1.0f / D_);
          const float var = fmaxf(ssq * (1.0f / D_) - mean * mean, 0.f);
          const float r1 = __builtin_amdgcn_rsqf(var + 1e-5f);
          const float r12 = r1 * __builtin_amdgcn_rsqf(var * r1 * r1 + 1e-5f);
          const float mr = -mean * r12, mr1 = -mean * r1;
#pragma unroll
          for (int tt = 0; tt < 2; ++tt) {
            const int cl = 32 * kh + 16 * tt + 4 * g;
            f32x4 c0 = *(const f32x4*)(cs2 + cl), c1 = *(const f32x4*)(cs2 + 64 + cl);
            float hv[4];
#pragma unroll
            for (int r = 0; r < 4; ++r) hv[r] = gelu_tanh(__builtin_fmaf(acc[tt][r], r12, mr * (c0[r] + c1[r])));
            res[tt][0] = pack2(hv[0], hv[1]); res[tt][1] = pack2(hv[2], hv[3]);
            xnr[tt][0] = pack2(__builtin_fmaf(bflo(x2r[tt][0]), r1, mr1), __builtin_fmaf(bfhi(x2r[tt][0]), r1, mr1));
            xnr[tt][1] = pack2(__builtin_fmaf(bflo(x2r[tt][1]), r1, mr1), __builtin_fmaf(bfhi(x2r[tt][1]), r1, mr1));
            asm volatile("" : "+v"(x2r[tt]), "+v"(xnr[tt]));
          }
        } else {
#pragma unroll
          for (int tt = 0; tt < 2; ++tt) {
            res[tt][0] = pack2(bflo(xnr[tt][0]) + acc[tt][0], bfhi(xnr[tt][0]) + acc[tt][1]);
            res[tt][1] = pack2(bflo(xnr[tt][1]) + acc[tt][2], bfhi(xnr[tt][1]) + acc[tt][3]);
            x1r[tt] = res[tt]; asm volatile("" : "+v"(x1r[tt]));
          }
        }
        u32x4 u = pair16(res[0], res[1]);
        st16(rs_ws, (ph == 0 ? x2_off : ph == 1 ? h_off : x1_off) + st_frag, u);
        if (ph != 1) put_stats(rs_ws, (ph == 0 ? sx2_off : sx1_off) + (unsigned)(trow64 >> 4) * 1792u + j * 256 + tok * 16 + kh * 8, res[0], res[1], g);
      }
      publish(fl + (2 + ph) * 256 + id, tag0 + 2 + ph);
      if (wv) {
        if (ph == 0) stage_w56<8>(a.w2, D_, 64 * j, 0, ldsb + 57344, 8 * (wv - 1), lane);
        else if (act6) {
          if (ph == 1) {
            stage_w56<8>(a.w3, F_, 128 * h, 0, ldsb, 8 * (wv - 1), lane);
            if (wv < 5) stage_w56<4>(a.w3, F_, 128 * h + 64, 0, ldsb + 73728, 40 + 4 * (wv - 1), lane);
          } else if (wv < 6) stage_w56<8>(a.w3, F_, 128 * h + 64, 0, ldsb + 73728, 8 * (wv - 1), lane);
        }
      }
      if (ph == 2 && !act6) {
        Dma56 dm4; dm4.init(a.w4, D_, 64 * j, 0, ldsb, lane);
#pragma unroll
        for (int t = 0; t < 16; ++t) { const int sl = 2 * (wv & 1) + (t >> 3); dm4.issue2((wv >> 1) * 56 + W4CHUNK((wv >> 1) & 1, sl) * 8 + (t & 7), 16 * wv + t); }
      }
    }
    }
    if (act6) {
      LANEVARS
      float* cs = (float*)(lds + 57344);
      const int kk = wv & 1;
      const unsigned ba = x1_off + (unsigned)((trow128 >> 4) * 14 + kk * 7) * 1024u + lane * 16;
      const unsigned bb2 = x1_off + (unsigned)(((trow128 >> 4) ^ 1) * 14 + kk * 7) * 1024u + lane * 16;
      const unsigned so = sx1_off + (unsigned)(trow128 >> 4) * 1792u + g * 256 + tok * 16;
      const unsigned so2 = padlane ? OOB : so + 1024;
      int toff[4];
#pragma unroll
      for (int t = 0; t < 4; ++t) toff[t] = (kk * 224 + 8 * g + q) * 128 + (((2 * t + (p >> 1)) ^ swA) << 4) + (p & 1) * 8;
      asm volatile("" :: "v"(ba), "v"(bb2), "v"(so), "v"(so2));
      WAIT_VM(0);
      BAR();
      {
        const int o = (kh ? 73728 : 0) + (8 * g + q) * 128 + (((2 * tg + (p >> 1)) ^ swA) << 4) + (p & 1) * 8;
        f32x4 cc = colsum_tile<14, 128>(lds + o, ones);
        if (tok == 0) *(f32x4*)(cs + 16 * wv + 4 * g) = cc;
      }
      wait_flags<1>(fl + 4 * 256 + panel * 16, 16, 1, tag0 + 4, wv, lane);
      bf16x8 xa[7], xb[7];
#pragma unroll
      for (int s = 0; s < 7; ++s) { xa[s] = ld16(rs_ws, ba + 1024 * s, 0); xb[s] = ld16(rs_ws, bb2 + 1024 * s, 0); }
      u32x4 sa = ld16u(rs_ws, so), sb = ld16u(rs_ws, so2);
      f32x4 csr[8];
#pragma unroll
      for (int t = 0; t < 8; ++t) csr[t] = *(const f32x4*)(cs + 16 * t + 4 * g);
      float rstd = 0.f, mr = 0.f;
      const unsigned tb = h3_off + (unsigned)(trow128 >> 4) * (56u * 1024u) + tok * 16;
      Dma56 dm4; dm4.init(a.w4, D_, 64 * j, 0, ldsb, lane);
      auto ring_piece = [&](int n) { dm4.issue2((n >> 5) * 56 + W4CHUNK((n >> 5) & 1, (n >> 3) & 3) * 8 + (n & 7), n); };
#pragma unroll
      for (int hf = 0; hf < 2; ++hf) {
        f32x4 acc[4], acb[4];
#pragma unroll
        for (int t = 0; t < 4; ++t) { acc[t] = (f32x4){0.f, 0.f, 0.f, 0.f}; acb[t] = acc[t]; }
        if (hf == 0) {
          gemm_loop2<4, 7, 128>(lds, toff, xa, xb, acc, acb);
          float ssum = 0.f, ssq = 0.f;
          get_stats(sa, sb, ssum, ssq);
          const float mean = ssum * (1.0f / D_);
          const float var = fmaxf(ssq * (1.0f / D_) - mean * mean, 0.f);
          rstd = __builtin_amdgcn_rsqf(var + 1e-5f);
          mr = -mean * rstd;
        } else {
          gemm_loop2<4, 7, 128>(lds + 73728, toff, xa, xb, acc, acb, [&](int s) {
            if (act64) { ring_piece(9 * wv + s); if (s < 2) ring_piece(9 * wv + 7 + s); }
          });
          WAIT_VM(9);
          if (!act64) WAIT_VM(0);
          BAR();
          if (threadIdx.x == 0) __hip_atomic_store(fl + 5 * 256 + id, tag0 + 5, RLX_AGENT);
        }
        {
          f32x4* mine = (f32x4*)(scr + wv * 4096);
#pragma unroll
          for (int t = 0; t < 4; ++t) mine[t * 64 + lane] = acb[t];
        }
        BAR();
        {
          const f32x4* other = (const f32x4*)(scr + (wv ^ 1) * 4096);
#pragma unroll
          for (int t = 0; t < 4; ++t) acc[t] += other[t * 64 + lane];
        }
#pragma unroll
        for (int pr = 0; pr < 2; ++pr) {
          u32x2 res[2];
#pragma unroll
          for (int tt = 0; tt < 2; ++tt) {
            const int t = 2 * pr + tt;
            if (hf == 1 && act64) { ring_piece(72 + 7 * wv + 2 * t); if (t < 3) ring_piece(72 + 7 * wv + 2 * t + 1); }
            float hv[4];
#pragma unroll
            for (int r = 0; r < 4; ++r) hv[r] = gelu_tanh(__builtin_fmaf(acc[t][r], rstd, mr * csr[4 * hf + t][r]));
            res[tt][0] = pack2(hv[0], hv[1]); res[tt][1] = pack2(hv[2], hv[3]);
          }
          u32x4 u = pair16(res[0], res[1]);
          const int col = 128 * h + 64 * hf + 32 * pr + 16 * (g & 1) + 8 * (g >> 1);
          st16(rs_ws, tb + (unsigned)((col >> 5) * 1024 + ((col >> 3) & 3) * 256), u);
        }
      }
      publish(fl + 7 * 256 + id, tag0 + 7);
    }
    if (!act64 && pair == 0) {
      LANEVARS
      wait_flags<0>(flags + 16 * 256 + h * 16, 1, 1, tag0 + 5, wv, lane);
      if (wv) stage_img(a.ws + OFF_IMG + h * 86016, ldsb, 12 * (wv - 1), lane);
      WAIT_VM(0);
      BAR();
    }
    if (act64) {
      LANEVARS
      const int t2 = wv & 1, kq = wv >> 1;
      char* ring = lds + kq * 32768;
      int toff[4];
#pragma unroll
      for (int t = 0; t < 4; ++t) toff[t] = (8 * g + q) * 128 + (((2 * t + (p >> 1)) ^ swA) << 4) + (p & 1) * 8;
      const unsigned ba = h3_off + (unsigned)((m * 4 + 2 * t2) * 56 + kq * 14) * 1024u + lane * 16;
      const char* imgsrc = a.ws + OFF_IMG + h * 86016 + lane * 16;
      asm volatile("" :: "v"(ba), "v"(toff[0]), "v"(toff[1]), "v"(toff[2]), "v"(toff[3]));
      const int pk = kq & 1;
      Dma56 dmr; dmr.init(a.w4, D_, 64 * j, 0, ldsb, lane);
      wait_flags<2>(fl + 5 * 256 + panel * 16, 16, 1, tag0 + 5, wv, lane, pair == 0 ? flags + 16 * 256 + h * 16 : nullptr);
      bf16x8 xa[14], xb[14];
      auto ldx = [&](int i) {
        const unsigned so = (unsigned)W4CHUNK(pk, i) * 2048u;
#pragma unroll
        for (int s = 0; s < 2; ++s) { xa[2 * i + s] = ld16(rs_ws, ba + 1024 * s, so); xb[2 * i + s] = ld16(rs_ws, ba + 56 * 1024 + 1024 * s, so); }
      };
      ldx(0); ldx(1); ldx(2);
      __builtin_amdgcn_sched_barrier(0);
      f32x4 acc[4], acb[4];
#pragma unroll
      for (int t = 0; t < 4; ++t) { acc[t] = (f32x4){0.f, 0.f, 0.f, 0.f}; acb[t] = acc[t]; }
#pragma unroll
      for (int i = 0; i < 7; ++i) {
        if (i == 4) WAIT_VM(4);
        else if (i == 5) { if (pair == 0) WAIT_VM(2); else WAIT_VM(0); }
        if (i >= 4) BAR();
        if (i == 4) {
#pragma unroll
          for (int s = 8; s < 14; ++s) { asm volatile("" : "+v"(xa[s])); asm volatile("" : "+v"(xb[s])); }
        }
        if (i >= 4 && pair == 0) {
          const int sl = (i == 4) ? 3 : i - 5;
#pragma unroll
          for (int qq = 0; qq < 3; ++qq) {
            const int n = qq * 32 + sl * 8 + wv;
            if (n < 84) glds16a_sc1(imgsrc + n * 1024, ldsb + (unsigned)n * 1024u);
          }
        }
        const char* Wimg = ring + (i & 3) * 8192;
        {
          bf16x8 xs[2], xt[2];
#pragma unroll
          for (int s = 0; s < 2; ++s) { xs[s] = xa[2 * i + s]; xt[s] = xb[2 * i + s]; }
          gemm_loop2<4, 2, 128>(Wimg, toff, xs, xt, acc, acb);
        }
        if (i + 4 < 7) {
          if (i == 0) wait_flags<2, false>(fl + 7 * 256 + panel * 16, 16, 1, tag0 + 7, wv, lane);
          else BAR();
          const int rb = kq * 56 + W4CHUNK(pk, i + 4) * 8 + 4 * t2;
#pragma unroll
          for (int t = 0; t < 4; ++t) dmr.issue2(rb + t, kq * 32 + (i & 3) * 8 + 4 * t2 + t);
        }
        if (i == 0) {
          ldx(3); ldx(4); ldx(5); ldx(6);
          __builtin_amdgcn_sched_barrier(0);
        }
      }
      BAR();
      if (pair == 0) {
#pragma unroll
        for (int qq = 0; qq < 3; ++qq) {
          const int n = qq * 32 + 16 + wv;
          if (n < 84) glds16a_sc1(imgsrc + n * 1024, ldsb + (unsigned)n * 1024u);
        }
      }
      {
        f32x4* mine = (f32x4*)(lds + 86016 + wv * 8192);
#pragma unroll
        for (int t = 0; t < 4; ++t) { mine[t * 64 + lane] = acc[t]; mine[(4 + t) * 64 + lane] = acb[t]; }
      }
      BAR();
      {
        const f32x4* src = (const f32x4*)(lds + 86016 + (tg >> 1) * 8192) + ((tg & 1) * 4 + 2 * kh) * 64 + lane;
        acc[0] = (src[0] + src[1024]) + (src[2048] + src[3072]);
        acc[1] = (src[64] + src[1024 + 64]) + (src[2048 + 64] + src[3072 + 64]);
#pragma unroll
        for (int tt = 0; tt < 2; ++tt) {
          xres[tt][0] = pack2(bflo(x2r[tt][0]) + (bflo(x1r[tt][0]) + acc[tt][0]), bfhi(x2r[tt][0]) + (bfhi(x1r[tt][0]) + acc[tt][1]));
          xres[tt][1] = pack2(bflo(x2r[tt][1]) + (bflo(x1r[tt][1]) + acc[tt][2]), bfhi(x2r[tt][1]) + (bfhi(x1r[tt][1]) + acc[tt][3]));
        }
        u32x4 u = pair16(xres[0], xres[1]);
        if (pair == 0) {
          st16(rs_ws, xp_off + st_frag, u);
          put_stats(rs_ws, sxp_off + (unsigned)(trow64 >> 4) * 1792u + j * 256 + tok * 16 + kh * 8, xres[0], xres[1], g);
          asm volatile("" : "+v"(xres[0]), "+v"(xres[1]));
          publish(fl + 6 * 256 + id, tag0 + 6);
        } else {
          __builtin_amdgcn_raw_buffer_store_b128(u, rs_out, (unsigned)(trow64 * D_ + colu) * 2u, 0, 16);
        }
      }
    }
  }
  if (blockIdx.x == 0 && threadIdx.x == 0) __hip_atomic_store((gu64*)a.ws, E + 64, RLX_AGENT);
}

extern "C" void kernel_launch(void* const* d_in, const int* in_sizes, int n_in,
                              void* d_out, int out_size, void* d_ws, size_t ws_size,
                              hipStream_t stream) {
  Args a;
  a.x0 = (const u16*)d_in[0];
  a.wqkv = (const u16*)d_in[1];
  a.wphi = (const u16*)d_in[2];
  a.wo = (const u16*)d_in[3];
  a.w1 = (const u16*)d_in[5];
  a.w2 = (const u16*)d_in[6];
  a.w3 = (const u16*)d_in[7];
  a.w4 = (const u16*)d_in[8];
  a.out = (u16*)d_out;
  a.ws = (char*)d_ws;
  mega<<<256, 512, 0, stream>>>(a);
}
```
